```python
import torch

def _softmax(x, axis=-1):
    return torch.softmax(x, dim=axis)
def _gelu(x):
    return torch.nn.functional.gelu(x, approximate="tanh")
def _silu(x):
    return torch.nn.functional.silu(x)
def _sigmoid(x):
    return torch.sigmoid(x)
def _layernorm(x, axis=-1, eps=1e-5):
    m = x.mean(dim=axis, keepdim=True)
    v = x.var(dim=axis, keepdim=True, unbiased=False)
    return (x - m) / torch.sqrt(v + eps)
def _scatter_add(tgt, idx, src, axis=0):
    return tgt.clone().index_add_(axis, idx, src)
def _segment_sum(src, seg, n):
    out = torch.zeros((n,) + src.shape[1:], dtype=src.dtype)
    return out.index_add_(0, seg, src)
def _pad(x, pw):
    flat = tuple(v for pair in reversed(pw) for v in pair)
    return torch.nn.functional.pad(x, flat)

kernel_name = 'standalone__sliding_window_attn__prompt'

def setup_inputs():

    _w = (lambda a: (lambda t: t.to(torch.bfloat16) if t.is_floating_point() else t)(torch.as_tensor(a)))
    B, H, S, D = 1, 32, 2048, 192
    return {
        "q": _w((lambda *s: torch.randn(*s, dtype=torch.bfloat16) * 0.8)(B, H, S, D)),
        "k": _w((lambda *s: torch.randn(*s, dtype=torch.bfloat16) * 0.8)(B, H, S, D)),
        "v": _w((lambda *s: torch.randn(*s, dtype=torch.bfloat16) * 0.05)(B, H, S, D)),
        "window": 256,
    }

def reference(q, k, v, window):
    B, H, S, D = q.shape
    scale = D ** -0.5
    scores = torch.einsum("bhqd,bhkd->bhqk", q, k) * scale
    idx = torch.arange(S)
    mask = (idx[:, None] - idx[None, :] >= 0) & (idx[:, None] - idx[None, :] < window)
    scores = torch.where(mask[None, None], scores, -1e9)
    p = _softmax(scores, axis=-1)
    return torch.einsum("bhqk,bhkd->bhqd", p, v)
```

```cpp
#include <hip/hip_runtime.h>
#include <hip/hip_bf16.h>
#include <stdint.h>

typedef short bf16x8 __attribute__((ext_vector_type(8)));
typedef short s16x4 __attribute__((ext_vector_type(4)));
typedef float f32x16 __attribute__((ext_vector_type(16)));
typedef float f32x2 __attribute__((ext_vector_type(2)));
typedef __bf16 bf16x2 __attribute__((ext_vector_type(2)));
typedef unsigned u32x4 __attribute__((ext_vector_type(4)));
typedef __attribute__((address_space(3))) char lds_char;
typedef __attribute__((address_space(3))) bf16x8 lds_bf16x8;
typedef __attribute__((address_space(3))) u32x4 lds_u32x4;
typedef __attribute__((address_space(3))) s16x4 lds_s16x4;

constexpr int SEQ = 2048, HD = 192, ROWB = HD * 2;
constexpr int TILEB = 32 * ROWB;
constexpr int UNITB = 2 * TILEB;
constexpr int NSLOT = 6;
constexpr int LDS_STAGE = NSLOT * UNITB;
constexpr int LDS_BYTES = LDS_STAGE + TILEB;
constexpr float C2 = 0.07216878364870322f * 1.4426950408889634f;
constexpr float THR = 8.0f;

#define DI __device__ __forceinline__
#define MFMA(a, b, c) __builtin_amdgcn_mfma_f32_32x32x16_bf16(a, b, c, 0, 0, 0)

DI unsigned rfl(unsigned x) { return __builtin_amdgcn_readfirstlane(x); }
DI unsigned cvtpk(float lo, float hi) {
    f32x2 v = {lo, hi}; bf16x2 b = __builtin_convertvector(v, bf16x2); return __builtin_bit_cast(unsigned, b);
}
DI unsigned swz(unsigned r) { return (((r >> 1) & 1u) << 2) | ((r >> 2) & 3u); }

template <bool NT>
DI void dma3(const char* sbase, unsigned m0v, unsigned v0, unsigned v1, unsigned v2) {
    unsigned keep;
    if constexpr (NT)
        asm volatile("s_mov_b32 %0, m0\n\ts_mov_b32 m0, %1\n\ts_nop 2\n\t"
                     "global_load_lds_dwordx4 %2, %5 nt\n\t"
                     "global_load_lds_dwordx4 %3, %5 offset:1024 nt\n\t"
                     "global_load_lds_dwordx4 %4, %5 offset:2048 nt\n\t"
                     "s_mov_b32 m0, %0"
                     : "=&s"(keep) : "s"(m0v), "v"(v0), "v"(v1), "v"(v2), "s"(sbase) : "memory");
    else
        asm volatile("s_mov_b32 %0, m0\n\ts_mov_b32 m0, %1\n\ts_nop 2\n\t"
                     "global_load_lds_dwordx4 %2, %5\n\t"
                     "global_load_lds_dwordx4 %3, %5 offset:1024\n\t"
                     "global_load_lds_dwordx4 %4, %5 offset:2048\n\t"
                     "s_mov_b32 m0, %0"
                     : "=&s"(keep) : "s"(m0v), "v"(v0), "v"(v1), "v"(v2), "s"(sbase) : "memory");
}
#define VMW(n) asm volatile("s_waitcnt vmcnt(" #n ")" ::: "memory")
DI void wait_vm3(int n) {
    if (n >= 5) {
        if (n >= 8) { if (n >= 9) VMW(27); else VMW(24); }
        else if (n == 7) VMW(21);
        else if (n == 6) VMW(18);
        else VMW(15);
    } else {
        if (n >= 3) { if (n == 4) VMW(12); else VMW(9); }
        else if (n == 2) VMW(6);
        else if (n == 1) VMW(3);
        else VMW(0);
    }
}

__global__ __launch_bounds__(512, 2)
void swa_fwd_kernel(const char* __restrict__ Q, const char* __restrict__ K, const char* __restrict__ V,
                    const int* __restrict__ Wp, char* __restrict__ O) {
    __shared__ __attribute__((aligned(1024))) char lds_mem[LDS_BYTES];
    lds_char* lds = (lds_char*)lds_mem;
    const unsigned lds0 = (unsigned)(uintptr_t)lds;
    const int tid = threadIdx.x, lane = tid & 63, r32 = lane & 31, hi = lane >> 5;
    const int p = rfl(tid >> 6);
    const int bid = blockIdx.x, nb8 = gridDim.x >> 3;
    const int vb = (bid & 7) * nb8 + (bid >> 3);
    const int head = vb >> 3, qb = vb & 7;
    const size_t hoff = (size_t)head * SEQ * ROWB;
    const int q0 = qb * 256;
    int W = *Wp;

    if (W <= 0) {
        float* red = (float*)lds_mem;
        const int c = tid % 96, part = tid / 96;
        float s0 = 0.f, s1 = 0.f;
        if (part < 5)
            for (int k = part; k < SEQ; k += 5) {
                const unsigned x = *(const unsigned*)(V + hoff + (size_t)k * ROWB + c * 4);
                s0 += __uint_as_float(x << 16); s1 += __uint_as_float(x & 0xffff0000u);
            }
        if (part < 5) { red[(part * 96 + c) * 2] = s0; red[(part * 96 + c) * 2 + 1] = s1; }
        __syncthreads();
        if (part < 5) {
            s0 = 0.f; s1 = 0.f;
            for (int j = 0; j < 5; ++j) { s0 += red[(j * 96 + c) * 2]; s1 += red[(j * 96 + c) * 2 + 1]; }
            const unsigned o = cvtpk(s0 * (1.f / SEQ), s1 * (1.f / SEQ));
            for (int r = part; r < 256; r += 5) *(unsigned*)(O + hoff + (size_t)(q0 + r) * ROWB + c * 4) = o;
        }
        return;
    }
    if (W > SEQ) W = SEQ;

    const int lowkey = q0 - W + 1;
    const int t_hi = qb * 8 + 7, t_lo = lowkey > 0 ? (lowkey >> 5) : 0;
    const int NT = t_hi - t_lo + 1, NU = NT + 4;
    const bool asc = (qb & 1) && lowkey >= 0;
    const int w = asc ? p : 7 - p;
    const int row0 = q0 + 32 * w;
    int endp;
    if (asc) endp = NT - 8 + p;
    else { const int lk = row0 - W + 1; endp = t_hi - (lk > 0 ? (lk >> 5) : 0); }

    unsigned voff[3];
#pragma unroll
    for (int j = 0; j < 3; ++j) {
        const unsigned line = 8 * j + (lane >> 3), r = 8 * (p & 3) + line / 3;
        voff[j] = (lane >> 3) * 128 + (((lane & 7) ^ swz(r)) << 4);
    }
    const char* kvsrc = (p < 4 ? K : V) + hoff + (p & 3) * 3072;
    const char* qsrc = Q + hoff + (size_t)q0 * ROWB + p * 3072;
    const unsigned m0base = lds0 + p * 3072;

    int uiss = 0, islot = 0;
    auto issue = [&]() {
        const int u = uiss;
        const unsigned m0v = m0base + islot * UNITB;
        if (u < 12 && (u % 3) == 0) {
            const int m = u / 3;
            dma3<true>(qsrc + (asc ? m : 3 - m) * UNITB, m0v, voff[0], voff[1], voff[2]);
        } else {
            const int i = u < 12 ? 2 * (u / 3) + (u % 3) - 1 : u - 4;
            const int t = asc ? t_lo + i : t_hi - i;
            dma3<false>(kvsrc + (size_t)t * TILEB, m0v, voff[0], voff[1], voff[2]);
        }
        ++uiss; islot = islot == NSLOT - 1 ? 0 : islot + 1;
    };
#pragma unroll
    for (int u = 0; u < NSLOT; ++u) issue();

    const unsigned kb0 = r32 * ROWB + ((hi ^ swz(r32)) << 4);
    const unsigned qq = (lane & 15) >> 2, pp = lane & 3, gb = (lane >> 4) & 1;
    const unsigned vb0 = TILEB + (4 * hi + qq) * ROWB + 64 * (qq >> 1) + 32 * gb + 16 * ((pp >> 1) ^ hi) + 8 * (pp & 1);

    int ustore = -1;
    int cslot = 0;
    auto head_step = [&](int i) {
        const int ui = i < 8 ? i + (i >> 1) + 1 : i + 4;
        wait_vm3(uiss - ui - 1 + (ui < ustore ? 4 : 0));
        asm volatile("s_waitcnt lgkmcnt(0)\n\ts_barrier" ::: "memory");
        const bool qstep = i < 8 && !(i & 1);
        int lim = (qstep ? ui - 1 : ui) + NSLOT; lim = lim < NU ? lim : NU;
        while (uiss < lim) issue();
        cslot += (qstep && i) ? 2 : 1; cslot = cslot >= NSLOT ? cslot - NSLOT : cslot;
    };

    bf16x8 qf[12];
    for (int i = 0; i <= p; ++i) {
        head_step(i);
        if (i == (p & ~1)) {
            const int qs = cslot == 0 ? NSLOT - 1 : cslot - 1;
            const unsigned a = qs * UNITB + (w & 1) * TILEB + kb0;
#pragma unroll
            for (int k = 0; k < 12; ++k)
                qf[k] = *(const lds_bf16x8*)(lds + ((a ^ ((k & 3) << 5)) + (k >> 2) * 128));
        }
    }
    f32x16 o[6];
#pragma unroll
    for (int d = 0; d < 6; ++d) o[d] = f32x16{};
    float mref = -1e30f, lsum = 0.f;

    for (int i = p; i <= endp; ++i) {
        const int t = asc ? t_lo + i : t_hi - i;
        const unsigned sb = cslot * UNITB;
        f32x16 s = f32x16{};
        {
            const unsigned a = sb + kb0;
#pragma unroll
            for (int k = 0; k < 12; ++k) {
                const bf16x8 kf = *(const lds_bf16x8*)(lds + ((a ^ ((k & 3) << 5)) + (k >> 2) * 128));
                s = MFMA(kf, qf[k], s);
            }
        }
        const int dq = row0 - 32 * t;
        if (dq < 31 || dq + 31 >= W) {
            const int dl = dq + r32 - 4 * hi;
#pragma unroll
            for (int e = 0; e < 16; ++e) {
                const int c = (e & 3) + 8 * (e >> 2);
                if ((unsigned)(dl - c) >= (unsigned)W) s[e] = -__builtin_inff();
            }
        }
        float mx = __builtin_fmaxf(__builtin_fmaxf(s[0], s[1]), s[2]);
#pragma unroll
        for (int e = 3; e < 15; e += 2) mx = __builtin_fmaxf(__builtin_fmaxf(mx, s[e]), s[e + 1]);
        mx = __builtin_fmaxf(mx, s[15]);
        { auto rr = __builtin_amdgcn_permlane32_swap(__float_as_uint(mx), __float_as_uint(mx), false, false);
          mx = __builtin_fmaxf(__uint_as_float(rr[0]), __uint_as_float(rr[1])); }
        mx *= C2;
        if (__builtin_expect(__any(mx - mref > THR), 0)) {
            const float mn = __builtin_fmaxf(mref, mx);
            const float al = __builtin_amdgcn_exp2f(mref - mn);
            mref = mn; lsum *= al;
#pragma unroll
            for (int d = 0; d < 6; ++d)
#pragma unroll
                for (int e = 0; e < 16; ++e) o[d][e] *= al;
        }
        float ps = 0.f;
#pragma unroll
        for (int e = 0; e < 16; ++e) { s[e] = __builtin_amdgcn_exp2f(__builtin_fmaf(s[e], C2, -mref)); ps += s[e]; }
        lsum += ps;
        u32x4 pw0 = {cvtpk(s[0], s[1]), cvtpk(s[2], s[3]), cvtpk(s[4], s[5]), cvtpk(s[6], s[7])};
        u32x4 pw1 = {cvtpk(s[8], s[9]), cvtpk(s[10], s[11]), cvtpk(s[12], s[13]), cvtpk(s[14], s[15])};
        const bf16x8 pf0 = __builtin_bit_cast(bf16x8, pw0), pf1 = __builtin_bit_cast(bf16x8, pw1);
        const unsigned va = sb + vb0;
#pragma unroll
        for (int d = 0; d < 6; ++d) {
#pragma unroll
            for (int ks = 0; ks < 2; ++ks) {
                const unsigned a0 = (va ^ ((d & 1) << 6)) + (d >> 1) * 128 + ks * 16 * ROWB;
                const unsigned a1 = (va ^ ((d & 1) << 6) ^ 32) + (d >> 1) * 128 + ks * 16 * ROWB + 8 * ROWB;
                const s16x4 lo = __builtin_amdgcn_ds_read_tr16_b64_v4i16((lds_s16x4*)(lds + a0));
                const s16x4 hh = __builtin_amdgcn_ds_read_tr16_b64_v4i16((lds_s16x4*)(lds + a1));
                const bf16x8 vf = __builtin_shufflevector(lo, hh, 0, 1, 2, 3, 4, 5, 6, 7);
                o[d] = MFMA(vf, ks ? pf1 : pf0, o[d]);
            }
        }
        if (i + 1 < NT) head_step(i + 1);
    }
    {
        unsigned stg = LDS_STAGE;
        if (endp == NT - 1) { int ss = cslot + 1 + (p >> 1); ss = ss >= NSLOT ? ss - NSLOT : ss; stg = ss * UNITB + (p & 1) * TILEB; }
        { auto rr = __builtin_amdgcn_permlane32_swap(__float_as_uint(lsum), __float_as_uint(lsum), false, false);
          lsum = __uint_as_float(rr[0]) + __uint_as_float(rr[1]); }
        const float inv = 1.0f / lsum;
        const unsigned wa = stg + r32 * ROWB + ((hi ^ (r32 & 7)) << 4);
#pragma unroll
        for (int d = 0; d < 6; ++d) {
#pragma unroll
            for (int g = 0; g < 2; ++g) {
                const int e = 8 * g;
                unsigned a0 = cvtpk(o[d][e] * inv, o[d][e + 1] * inv), a1 = cvtpk(o[d][e + 2] * inv, o[d][e + 3] * inv);
                unsigned b0 = cvtpk(o[d][e + 4] * inv, o[d][e + 5] * inv), b1 = cvtpk(o[d][e + 6] * inv, o[d][e + 7] * inv);
                auto r0 = __builtin_amdgcn_permlane32_swap(a0, b0, false, false);
                auto r1 = __builtin_amdgcn_permlane32_swap(a1, b1, false, false);
                const u32x4 val = {r0[0], r1[0], r0[1], r1[1]};
                *(lds_u32x4*)(lds + ((wa ^ ((((4 * d + 2 * g) & 7)) << 4)) + (d >> 1) * 128)) = val;
            }
        }
        asm volatile("s_waitcnt lgkmcnt(0)" ::: "memory");
        char* ob = O + hoff + (size_t)row0 * ROWB;
        u32x4 ov[12];
#pragma unroll
        for (int j = 0; j < 12; ++j) {
            const unsigned line = 8 * (j % 3) + (lane >> 3), rr = (line / 3) & 7;
            ov[j] = *(const lds_u32x4*)(lds + (stg + (j / 3) * 3072 + line * 128 + (((lane & 7) ^ rr) << 4)));
        }
        const unsigned so = lane * 16;
#pragma unroll
        for (int j = 0; j < 12; ++j)
            asm volatile("global_store_dwordx4 %0, %1, %2 nt\n\ts_nop 1" :: "v"(so), "v"(ov[j]), "s"(ob + j * 1024) : "memory");
        ustore = uiss;
    }
    for (int i = endp + 2; i < NT; ++i) head_step(i);
}

extern "C" void kernel_launch(void* const* d_in, const int* in_sizes, int n_in,
                              void* d_out, int out_size, void* d_ws, size_t ws_size,
                              hipStream_t stream) {
    (void)n_in; (void)out_size; (void)d_ws; (void)ws_size;
    const int heads = in_sizes[0] / (SEQ * HD);
    swa_fwd_kernel<<<dim3(heads * (SEQ / 256)), dim3(512), 0, stream>>>(
        (const char*)d_in[0], (const char*)d_in[1], (const char*)d_in[2], (const int*)d_in[3], (char*)d_out);
}
```

```cpp
#include <hip/hip_runtime.h>
#include <hip/hip_bf16.h>
#include <stdint.h>

typedef short bf16x8 __attribute__((ext_vector_type(8)));
typedef short s16x4 __attribute__((ext_vector_type(4)));
typedef float f32x16 __attribute__((ext_vector_type(16)));
typedef float f32x2 __attribute__((ext_vector_type(2)));
typedef __bf16 bf16x2 __attribute__((ext_vector_type(2)));
typedef unsigned u32x4 __attribute__((ext_vector_type(4)));
typedef __attribute__((address_space(3))) char lds_char;
typedef __attribute__((address_space(3))) bf16x8 lds_bf16x8;
typedef __attribute__((address_space(3))) u32x4 lds_u32x4;
typedef __attribute__((address_space(3))) s16x4 lds_s16x4;

constexpr int SEQ = 2048, HD = 192, ROWB = HD * 2;
constexpr int TILEB = 32 * ROWB;
constexpr int UNITB = 2 * TILEB;
constexpr int NSLOT = 6;
constexpr int LDS_MASK = NSLOT * UNITB;
constexpr int LDS_BYTES = LDS_MASK + 4 * 4096;
constexpr float C2 = 0.07216878364870322f * 1.4426950408889634f;
constexpr float THR = 8.0f;
constexpr float MSENT = -1e30f;

#define LPOL ""
#define QPOL "nt"
#define SPOL "sc1 nt"
#define DI __device__ __forceinline__
#define MFMA(a, b, c) __builtin_amdgcn_mfma_f32_32x32x16_bf16(a, b, c, 0, 0, 0)
#define SBAR() __builtin_amdgcn_sched_barrier(0)

DI unsigned rfl(unsigned x) { return __builtin_amdgcn_readfirstlane(x); }
DI unsigned cvtpk(float lo, float hi) {
    f32x2 v = {lo, hi}; bf16x2 b = __builtin_convertvector(v, bf16x2); return __builtin_bit_cast(unsigned, b);
}
DI unsigned swz(unsigned r) { return (((r >> 1) & 1u) << 2) | ((r >> 2) & 3u); }

typedef __amdgpu_buffer_rsrc_t srd_t;
template <int J, int LO, bool THROTTLE, bool ISQ = false>
DI void dma1(srd_t srd, unsigned soff, unsigned m0v, unsigned voff) {
    if constexpr (ISQ)
        asm volatile("s_add_u32 m0, %0, %3\n\ts_nop 2\n\ts_waitcnt vmcnt(5)\n\tbuffer_load_dwordx4 %1, %2, %5 offen offset:%4 " QPOL " lds"
                     :: "s"(m0v), "v"(voff), "s"(srd), "i"(LO + J * 3968), "i"(J * 128), "s"(soff) : "memory", "scc");
    else if constexpr (THROTTLE)
        asm volatile("s_add_u32 m0, %0, %3\n\ts_nop 2\n\ts_waitcnt vmcnt(5)\n\tbuffer_load_dwordx4 %1, %2, %5 offen offset:%4 " LPOL " lds"
                     :: "s"(m0v), "v"(voff), "s"(srd), "i"(LO + J * 3968), "i"(J * 128), "s"(soff) : "memory", "scc");
    else
        asm volatile("s_add_u32 m0, %0, %3\n\ts_nop 0\n\tbuffer_load_dwordx4 %1, %2, %5 offen offset:%4 " LPOL " lds"
                     :: "s"(m0v), "v"(voff), "s"(srd), "i"(LO + J * 3968), "i"(J * 128), "s"(soff) : "memory", "scc");
}
template <int LO, bool ISQ = false>
DI void dma3(srd_t srd, unsigned soff, unsigned m0v, unsigned voff) { dma1<0, LO, true, ISQ>(srd, soff, m0v, voff); dma1<1, LO, true, ISQ>(srd, soff, m0v, voff); dma1<2, LO, true, ISQ>(srd, soff, m0v, voff); }
DI srd_t make_srd(const char* p, unsigned bytes) { return __builtin_amdgcn_make_buffer_rsrc((void*)p, 0, bytes, 0x00020000); }
#define WAIT_BAR(n) asm volatile("s_waitcnt vmcnt(" #n ") lgkmcnt(0)\n\ts_barrier" ::: "memory")
#define PIN(x) asm volatile("" : "+v"(x))
#define PINS(x) asm volatile("" : "+s"(x))

__global__ __launch_bounds__(512, 2)
void swa_fwd_kernel(const char* __restrict__ Q, const char* __restrict__ K, const char* __restrict__ V,
                    const int* __restrict__ Wp, char* __restrict__ O) {
    __shared__ __attribute__((aligned(1024))) char lds_mem[LDS_BYTES];
    lds_char* lds = (lds_char*)lds_mem;
    const unsigned lds0 = (unsigned)(uintptr_t)lds;
    const int tid = threadIdx.x, lane = tid & 63, r32 = lane & 31, hi = lane >> 5;
    const int w = rfl(tid >> 6);
    const int bid = blockIdx.x, nb8 = gridDim.x >> 3;
    const int vb = (bid & 7) * nb8 + (bid >> 3);
    const int head = vb >> 3, qb = vb & 7;
    const size_t hoff = (size_t)head * SEQ * ROWB;
    const int q0 = qb * 256;
    int W = *Wp;
    constexpr int YDLY = 200;
    const bool late = head & 1;
    const unsigned voff = (lane >> 3) * ROWB + (((lane & 7) ^ swz(8 * (w & 3) + (lane >> 3))) << 4);
    const unsigned m0base = lds0 + (w >> 2) * TILEB + (w & 3) * 1024;
    const srd_t qsrd = make_srd(Q + hoff, SEQ * ROWB);
    const unsigned qoff = (q0 + w * 8) * ROWB;
    if (!late) {
        dma3<0, true>(qsrd, qoff, m0base, voff);
        dma3<UNITB, true>(qsrd, qoff + UNITB, m0base, voff);
    }
    const unsigned long long tstart = __builtin_amdgcn_s_memrealtime();
    if (W <= 0) {
        asm volatile("s_waitcnt vmcnt(0)" ::: "memory");
        __syncthreads();
        float* red = (float*)lds_mem;
        const int c = tid % 96, part = tid / 96;
        float s0 = 0.f, s1 = 0.f;
        if (part < 5)
            for (int k = part; k < SEQ; k += 5) {
                const unsigned x = *(const unsigned*)(V + hoff + (size_t)k * ROWB + c * 4);
                s0 += __uint_as_float(x << 16); s1 += __uint_as_float(x & 0xffff0000u);
            }
        if (part < 5) { red[(part * 96 + c) * 2] = s0; red[(part * 96 + c) * 2 + 1] = s1; }
        __syncthreads();
        if (part < 5) {
            s0 = 0.f; s1 = 0.f;
            for (int j = 0; j < 5; ++j) { s0 += red[(j * 96 + c) * 2]; s1 += red[(j * 96 + c) * 2 + 1]; }
            const unsigned o = cvtpk(s0 * (1.f / SEQ), s1 * (1.f / SEQ));
            for (int r = part; r < 256; r += 5) *(unsigned*)(O + hoff + (size_t)(q0 + r) * ROWB + c * 4) = o;
        }
        return;
    }
    if (W > SEQ) W = SEQ;

    int tv = (q0 - W + 1) >> 5; tv = tv < -7 ? -7 : tv;
    const int nt = qb * 8 - tv + 1;
    const int M = nt > 8 ? nt : 8;
    const int JA = tv < 0 ? 1 - tv : 1;
    const int JB = nt + 7 - M;
    auto srca = [&](int x) { return x >= JA && x < M ? (unsigned)(tv + x - 1) * TILEB + (w & 3) * 8 * ROWB : 0x80000000u; };
    auto srcb = [&](int x) { return x <= JB ? (unsigned)(tv + x - 1 + M) * TILEB + (w & 3) * 8 * ROWB : 0x80000000u; };
    const srd_t kvsrd = make_srd((w < 4 ? K : V) + hoff, SEQ * ROWB);
    const int row0 = q0 + 32 * w;
    const int medge = W - 31 > 32 ? (W - 31 + 31) & ~31 : 32;
    unsigned dtile, tsa, tsb, tm0;
    int last;
    {
        const int j = lane;
        const bool ea = j >= JA, eb = j <= JB;
        const bool usea = ea && j - 1 >= w && j - 1 < w + nt && j < M, useb = eb && j - 1 + M < w + nt;
        const int dq = row0 - 32 * (tv + j - 1 + (usea ? 0 : M));
        dtile = (unsigned)(2 * ((j + 2) % 3) + ((usea || (!useb && ea)) ? 0 : 1)) * UNITB
              | (!(usea || useb) ? 4u : dq == 0 ? 1u : dq == medge ? 2u : dq > medge ? 3u : 0u);
        last = 63 - __builtin_clzll(__ballot(usea || useb));
        const int x = w < 4 ? j + 3 : j + 2;
        tsa = srca(x); tsb = srcb(x);
        tm0 = x < M ? m0base + (unsigned)(2 * ((x + 2) % 3)) * UNITB : 0x30000u;
    }
    if (w < 4) {
        const int dl = (w == 0 ? 0 : w == 3 ? -64 : medge + 32 * (w - 1)) + r32 - 4 * hi;
#pragma unroll
        for (int g = 0; g < 4; ++g) {
            u32x4 z;
#pragma unroll
            for (int e = 0; e < 4; ++e) z[e] = (unsigned)(dl - 8 * g - e) >= (unsigned)W ? 0xff800000u : 0u;
            *(lds_u32x4*)(lds + (LDS_MASK + w * 4096 + g * 1024 + lane * 16)) = z;
        }
    }

    const unsigned kb0 = r32 * 128 + ((hi ^ swz(r32)) << 4);
    const unsigned qq = (lane & 15) >> 2, pp = lane & 3, gb = (lane >> 4) & 1;
    const unsigned vb0 = TILEB + (4 * hi + qq) * 128 + 64 * (qq >> 1) + 32 * gb + 16 * ((pp >> 1) ^ hi) + 8 * (pp & 1);

    f32x16 o[6];
#pragma unroll
    for (int d = 0; d < 6; ++d) { o[d] = f32x16{}; PIN(o[d]); }
    float mref = MSENT, lsum = 0.f;

    bf16x8 qf[12];
    {
        if (late) {
            while ((int)(__builtin_amdgcn_s_memrealtime() - tstart) < YDLY) __builtin_amdgcn_s_sleep(2);
            dma3<0, true>(qsrd, qoff, m0base, voff);
            dma3<UNITB, true>(qsrd, qoff + UNITB, m0base, voff);
        }
        dma3<2 * UNITB, true>(qsrd, qoff + 2 * UNITB, m0base, voff);
        dma3<3 * UNITB, true>(qsrd, qoff + 3 * UNITB, m0base, voff);
        WAIT_BAR(6);
        const unsigned a = w * TILEB + kb0;
        dma3<5 * UNITB>(kvsrd, srcb(0), m0base, voff);
        if (w >= 4) WAIT_BAR(3);
#pragma unroll
        for (int k = 0; k < 12; ++k) qf[k] = *(const lds_bf16x8*)(lds + ((a ^ ((k & 3) << 5)) + (k >> 2) * 4096));
        if (w < 4) {
            WAIT_BAR(3);
            dma3<0>(kvsrd, srca(1), m0base, voff);
            dma3<UNITB>(kvsrd, srcb(1), m0base, voff);
        }
    }
    f32x16 s, p;
    bf16x8 kf[12], vf[12];
    u32x4 pw0, pw1;
    float r0, r1, r2, r3, alpha;
    unsigned ka0, ka1, ka2, ka3, va, dt, dn, isa, isb, im0;
    int resc;
#define EX(e) p[e] = __builtin_amdgcn_exp2f(p[e])
#define FM(e) p[e] = __builtin_fmaf(s[e], C2, -mref)
#define KRD(k) kf[k] = *(const lds_bf16x8*)(lds + ((((k) & 3) == 0 ? ka0 : ((k) & 3) == 1 ? ka1 : ((k) & 3) == 2 ? ka2 : ka3) + ((k) >> 2) * 4096))
#define KADDR(d) do { ka0 = ((d) & ~31u) + kb0; ka1 = ka0 ^ 32; ka2 = ka0 ^ 64; ka3 = ka0 ^ 96; PIN(ka0); PIN(ka1); PIN(ka2); PIN(ka3); } while (0)
#define DTAB(j) do { isa = __builtin_amdgcn_readlane(tsa, j); isb = __builtin_amdgcn_readlane(tsb, j); im0 = __builtin_amdgcn_readlane(tm0, j); PINS(isa); PINS(isb); PINS(im0); } while (0)
#define VRD(k) do { const int ks_ = (k) / 6, d_ = (k) % 6;                                                                        \
            const unsigned a0_ = (va ^ ((d_ & 1) << 6)) + (d_ >> 1) * 4096 + ks_ * 16 * 128;                                      \
            const unsigned a1_ = (va ^ ((d_ & 1) << 6) ^ 32) + (d_ >> 1) * 4096 + ks_ * 16 * 128 + 8 * 128;                       \
            const s16x4 lo_ = __builtin_amdgcn_ds_read_tr16_b64_v4i16((lds_s16x4*)(lds + a0_));                                   \
            const s16x4 hh_ = __builtin_amdgcn_ds_read_tr16_b64_v4i16((lds_s16x4*)(lds + a1_));                                   \
            vf[k] = __builtin_shufflevector(lo_, hh_, 0, 1, 2, 3, 4, 5, 6, 7); } while (0)
#define PRIO(x) __builtin_amdgcn_s_setprio(x)
#define DMA(J) dma1<(J) % 3, (J) / 3 * UNITB, false>(kvsrd, (J) < 3 ? isa : isb, im0, voff)
#define QK(k) s = MFMA(kf[k], qf[k], s)
#define QK0() asm volatile("s_and_b32 vcc_lo, %3, 7\n\ts_cbranch_scc1 .Lmask%=\n\t"                                            \
                     "v_mfma_f32_32x32x16_bf16 %0, %1, %2, 0\n"                                                                  \
                     ".Ldone%=:\n\t.subsection 1\n"                                                                             \
                     ".Lmask%=:\n\tv_mfma_f32_32x32x16_bf16 %0, %1, %2, %0\n\ts_branch .Ldone%=\n\t.subsection 0"              \
                     : "+v"(s) : "v"(kf[0]), "v"(qf[0]), "s"(dt) : "scc", "vcc")
#define PV(k) o[(k) % 6] = MFMA(vf[k], __builtin_bit_cast(bf16x8, (k) < 6 ? pw0 : pw1), o[(k) % 6])
#define PK0() do { pw0 = u32x4{cvtpk(p[0], p[1]), cvtpk(p[2], p[3]), cvtpk(p[4], p[5]), cvtpk(p[6], p[7])}; PIN(pw0); } while (0)
#define PK1() do { pw1 = u32x4{cvtpk(p[8], p[9]), cvtpk(p[10], p[11]), cvtpk(p[12], p[13]), cvtpk(p[14], p[15])}; PIN(pw1); } while (0)
#define SUM0() asm volatile("v_add_f32 %0, %4, %5\n\tv_add_f32 %1, %6, %7\n\tv_add_f32 %2, %8, %9\n\tv_add_f32 %3, %10, %11"       \
                     : "=&v"(r0), "=&v"(r1), "=&v"(r2), "=&v"(r3) : "v"(p[0]), "v"(p[1]), "v"(p[2]), "v"(p[3]), "v"(p[4]), "v"(p[5]), "v"(p[6]), "v"(p[7]))
#define SUM1(e) asm volatile("v_add_f32 %0, %0, %4\n\tv_add_f32 %1, %1, %5\n\tv_add_f32 %2, %2, %6\n\tv_add_f32 %3, %3, %7"        \
                     : "+v"(r0), "+v"(r1), "+v"(r2), "+v"(r3) : "v"(p[e]), "v"(p[e + 1]), "v"(p[e + 2]), "v"(p[e + 3]))
#define SUM2() asm volatile("v_add_f32 %0, %0, %1\n\tv_add_f32 %2, %2, %3\n\tv_add_f32 %4, %4, %0\n\tv_add_f32 %4, %4, %2"         \
                     : "+v"(r0), "+v"(r1), "+v"(r2), "+v"(r3), "+v"(lsum))
#define MAX0() asm volatile("v_max3_f32 %0, %3, %4, %5\n\tv_max3_f32 %1, %6, %7, %8\n\tv_max3_f32 %2, %9, %10, %11\n\tv_max3_f32 %0, %0, %12, %13" \
                     : "=&v"(r0), "=&v"(r1), "=&v"(r2) : "v"(s[0]), "v"(s[1]), "v"(s[2]), "v"(s[3]), "v"(s[4]), "v"(s[5]), "v"(s[6]), "v"(s[7]), "v"(s[8]), "v"(s[9]), "v"(s[10]))
#define MAX1() asm volatile("v_max3_f32 %1, %1, %3, %4\n\tv_max3_f32 %2, %2, %5, %6\n\tv_max3_f32 %0, %0, %1, %7\n\tv_max_f32 %0, %0, %2" \
                     : "+v"(r0), "+v"(r1), "+v"(r2) : "v"(s[11]), "v"(s[12]), "v"(s[13]), "v"(s[14]), "v"(s[15]))
#define DECIDE() do { resc = 0;                                                                                                   \
        if (__builtin_expect(__any(__builtin_fmaf(r0, C2, -mref) > THR), 0)) {                                                    \
            auto rr_ = __builtin_amdgcn_permlane32_swap(__float_as_uint(r0), __float_as_uint(r0), false, false);                  \
            const float mn_ = __builtin_fmaxf(mref, __builtin_fmaxf(__uint_as_float(rr_[0]), __uint_as_float(rr_[1])) * C2);      \
            alpha = __builtin_amdgcn_exp2f(mref - mn_);                                                                           \
            resc = __any(mref != MSENT && mn_ != mref);                                                                           \
            mref = mn_; lsum *= alpha; } } while (0)
#define MASKRD(d) do { if (__builtin_expect((d) & 7, 0)) {                                                                        \
            const unsigned ma_ = LDS_MASK - 4096 + ((d) & 7) * 4096 + lane * 16;                                                  \
            _Pragma("unroll") for (int g = 0; g < 4; ++g) { const u32x4 z_ = *(const lds_u32x4*)(lds + (ma_ + g * 1024));         \
                _Pragma("unroll") for (int e = 0; e < 4; ++e) s[4 * g + e] = __uint_as_float(z_[e]); } } } while (0)

    dt = __builtin_amdgcn_readlane(dtile, 0); dn = __builtin_amdgcn_readlane(dtile, 1);
    KADDR(dt);
    isa = srca(w < 4 ? 2 : 1); isb = srcb(w < 4 ? 2 : 1); im0 = m0base + (w < 4 ? 2 * UNITB : 0);
    PINS(dt); PINS(dn); PINS(isa); PINS(isb); PINS(im0);
    MASKRD(dt); SBAR();
    WAIT_BAR(6);
    KRD(0); SBAR(); KRD(1); SBAR(); KRD(2); SBAR(); KRD(3); SBAR();
    QK0(); KRD(4); SBAR();
    QK(1); KRD(5); DMA(0); SBAR();
    QK(2); KRD(6); SBAR();
    QK(3); KRD(7); DMA(1); SBAR();
    QK(4); KRD(8); SBAR();
    QK(5); KRD(9); DMA(2); SBAR();
    QK(6); KRD(10); SBAR();
    QK(7); KRD(11); DMA(3); SBAR();
    QK(8); SBAR();
    QK(9); DMA(4); SBAR();
    QK(10); SBAR();
    QK(11); DMA(5); SBAR();
    KADDR(dn); DTAB(0);
    asm volatile("s_nop 7" : "+v"(s));
    MAX0(); MAX1(); DECIDE();
#pragma unroll
    for (int e = 0; e < 16; ++e) FM(e);
    PIN(p); SBAR();
    MASKRD(dn); SBAR();
#pragma unroll
    for (int e = 0; e < 6; ++e) EX(e);
    PIN(p); SBAR();
    WAIT_BAR(6);
    KRD(0); SBAR(); KRD(1); SBAR(); KRD(2); SBAR(); KRD(3); SBAR();
    DMA(0);
#pragma unroll
    for (int e = 6; e < 10; ++e) EX(e);
    PIN(p); SBAR();

    for (int j = 1; j <= last; ++j) {
        const unsigned dp = dt; dt = dn;
        QK0(); KRD(4); EX(10); EX(11); PIN(p); SBAR();
        QK(1); KRD(5); EX(12); EX(13); PIN(p); DMA(1); SBAR();
        QK(2); KRD(6); EX(14); EX(15); PIN(p); SBAR();
        QK(3); KRD(7); PK0(); PRIO(2); SBAR();
        QK(4); KRD(8); PK1(); SBAR();
        QK(5); KRD(9); SUM0(); DMA(2); SBAR();
        QK(6); KRD(10); SUM1(8); SBAR();
        QK(7); KRD(11); SUM1(12); va = (dp & ~31u) + vb0; PIN(va); SBAR();
        QK(8); VRD(0); SUM2(); SBAR();
        QK(9); VRD(1); DMA(3); PRIO(1); SBAR();
        QK(10); VRD(2); dn = __builtin_amdgcn_readlane(dtile, j + 1); PINS(dn); SBAR();
        QK(11); VRD(3); KADDR(dn); SBAR();
        PV(0); VRD(4); VRD(5); DMA(4); SBAR();
        PV(1); VRD(6); MAX0(); SBAR();
        PV(2); VRD(7); MAX1(); DECIDE(); SBAR();
        PV(3); VRD(8); FM(0); FM(1); FM(2); FM(3); PIN(p); PRIO(0); SBAR();
        PV(4); VRD(9); FM(4); FM(5); FM(6); FM(7); PIN(p); DMA(5); SBAR();
        PV(5); VRD(10); FM(8); FM(9); FM(10); FM(11); PIN(p); SBAR();
        PV(6); VRD(11); FM(12); FM(13); FM(14); FM(15); PIN(p); SBAR();
        PV(7); EX(0); EX(1); PIN(p); DTAB(j); SBAR();
        PV(8); EX(2); EX(3); PIN(p); SBAR();
        WAIT_BAR(6);
        PRIO(3);
        SBAR();
        PV(9); SBAR();
        MASKRD(dn); SBAR();
        KRD(0); SBAR(); KRD(1); EX(4); EX(5); PIN(p); SBAR();
        PV(10); KRD(2); KRD(3); EX(6); EX(7); PIN(p); SBAR();
        PV(11); EX(8); EX(9); PIN(p); DMA(0); SBAR();
        if (__builtin_expect(resc, 0)) {
#pragma unroll
            for (int d = 0; d < 6; ++d)
#pragma unroll
                for (int e = 0; e < 16; ++e) o[d][e] *= alpha;
        }
    }
    va = (dt & ~31u) + vb0;
    VRD(0); VRD(1); VRD(2); VRD(3); VRD(6); VRD(7); VRD(8); VRD(9); VRD(4); VRD(5); VRD(10); VRD(11); SBAR();
    for (int j = last + 1; j < M; ++j) {
        DMA(1); DMA(2); DMA(3); DMA(4); DMA(5);
        DTAB(j);
        WAIT_BAR(6);
        asm volatile("s_nop 4");
        DMA(0);
    }
    {
        unsigned stg = (unsigned)(2 * ((M + 2) % 3)) * UNITB + w * TILEB; stg = stg >= NSLOT * UNITB ? stg - NSLOT * UNITB : stg;
        const unsigned wa = stg + r32 * 128 + ((hi ^ (r32 & 7)) << 4);
        const unsigned ra = stg + (lane >> 3) * 128 + (((lane & 7) ^ (lane >> 3)) << 4);
        char* ob = O + hoff + (size_t)row0 * ROWB;
        const unsigned so = (lane >> 3) * ROWB + (lane & 7) * 16;
        float inv; u32x4 ov[4];
#define CV(c, g) do { const int d_ = 2 * (c) + ((g) >> 1), e_ = 8 * ((g) & 1);                                                    \
            const unsigned a0_ = cvtpk(o[d_][e_] * inv, o[d_][e_ + 1] * inv), a1_ = cvtpk(o[d_][e_ + 2] * inv, o[d_][e_ + 3] * inv);         \
            const unsigned b0_ = cvtpk(o[d_][e_ + 4] * inv, o[d_][e_ + 5] * inv), b1_ = cvtpk(o[d_][e_ + 6] * inv, o[d_][e_ + 7] * inv);     \
            auto s0_ = __builtin_amdgcn_permlane32_swap(a0_, b0_, false, false);                                                  \
            auto s1_ = __builtin_amdgcn_permlane32_swap(a1_, b1_, false, false);                                                  \
            *(lds_u32x4*)(lds + ((wa ^ ((g) << 5)) + (c) * 4096)) = u32x4{s0_[0], s1_[0], s0_[1], s1_[1]}; SBAR(); } while (0)
#define ORD(c) do { _Pragma("unroll") for (int g = 0; g < 4; ++g) ov[g] = *(const lds_u32x4*)(lds + (ra + (c) * 4096 + g * 1024)); SBAR(); } while (0)
#define OST(c) do { _Pragma("unroll") for (int g = 0; g < 4; ++g)                                                                 \
            asm volatile("global_store_dwordx4 %0, %1, %2 offset:%3 " SPOL "\n\ts_nop 1" :: "v"(so), "v"(ov[g]), "s"(ob + g * 8 * ROWB), "i"((c) * 128) : "memory"); SBAR(); } while (0)
        PK0(); SBAR();
        PV(0); EX(10); EX(11); EX(12); PIN(p); SBAR();
        PV(1); EX(13); EX(14); EX(15); PIN(p); SBAR();
        PV(2); PK1(); SUM0(); SBAR();
        PV(3); SUM1(8); SUM1(12); SBAR();
        PV(6); SUM2(); SBAR();
        PV(7);
        { auto rr = __builtin_amdgcn_permlane32_swap(__float_as_uint(lsum), __float_as_uint(lsum), false, false);
          inv = __builtin_amdgcn_rcpf(__uint_as_float(rr[0]) + __uint_as_float(rr[1])); PIN(inv); SBAR(); }
        PV(8); CV(0, 0);
        PV(9); CV(0, 1);
        PV(4); CV(0, 2);
        PV(5); CV(0, 3);
        PV(10); ORD(0); CV(1, 0);
        PV(11); CV(1, 1);
        OST(0); CV(1, 2); CV(1, 3); ORD(1);
        CV(2, 0); CV(2, 1); OST(1);
        CV(2, 2); CV(2, 3); ORD(2); OST(2);
    }
}

extern "C" void kernel_launch(void* const* d_in, const int* in_sizes, int n_in,
                              void* d_out, int out_size, void* d_ws, size_t ws_size,
                              hipStream_t stream) {
    (void)n_in; (void)out_size; (void)d_ws; (void)ws_size;
    const int heads = in_sizes[0] / (SEQ * HD);
    swa_fwd_kernel<<<dim3(heads * (SEQ / 256)), dim3(512), 0, stream>>>(
        (const char*)d_in[0], (const char*)d_in[1], (const char*)d_in[2], (const int*)d_in[3], (char*)d_out);
}
```
